```python
import torch

def _softmax(x, axis=-1):
    return torch.softmax(x, dim=axis)
def _gelu(x):
    return torch.nn.functional.gelu(x, approximate="tanh")
def _silu(x):
    return torch.nn.functional.silu(x)
def _sigmoid(x):
    return torch.sigmoid(x)
def _layernorm(x, axis=-1, eps=1e-5):
    m = x.mean(dim=axis, keepdim=True)
    v = x.var(dim=axis, keepdim=True, unbiased=False)
    return (x - m) / torch.sqrt(v + eps)
def _scatter_add(tgt, idx, src, axis=0):
    return tgt.clone().index_add_(axis, idx, src)
def _segment_sum(src, seg, n):
    out = torch.zeros((n,) + src.shape[1:], dtype=src.dtype)
    return out.index_add_(0, seg, src)
def _pad(x, pw):
    flat = tuple(v for pair in reversed(pw) for v in pair)
    return torch.nn.functional.pad(x, flat)

kernel_name = 'standalone__ragged_sequences_packed_sequences__ragged'

def setup_inputs():

    _w = (lambda a: (lambda t: t.to(torch.bfloat16) if t.is_floating_point() else t)(torch.as_tensor(a)))
    cu = torch.tensor([0, 400, 516, 964, 1456, 1702, 1900, 2021, 2266], dtype=torch.int64)
    T = int(cu[-1])
    H, D = 16, 128
    lens = torch.diff(cu)
    seg_ids = torch.repeat_interleave(torch.arange(len(lens), dtype=torch.int64), lens)
    return {
        "q": _w((lambda *s: torch.randn(*s, dtype=torch.bfloat16) * 0.8)(T, H, D)),
        "k": _w((lambda *s: torch.randn(*s, dtype=torch.bfloat16) * 0.8)(T, H, D)),
        "v": _w((lambda *s: torch.randn(*s, dtype=torch.bfloat16) * 0.05)(T, H, D)),
        "seg_ids": _w(seg_ids),
    }

def reference(q, k, v, seg_ids):
    T, H, D = q.shape
    scale = D ** -0.5
    same_seq = seg_ids[:, None] == seg_ids[None, :]
    logits = torch.einsum("qhd,khd->hqk", q, k) * scale
    bias = (1.0 - (lambda x, dt: x.to(dt))(same_seq, q.dtype)) * (-1e9)
    p = _softmax(logits + bias[None, :, :], axis=-1)
    return torch.einsum("hqk,khd->qhd", p, v)
```

```cpp
#include <hip/hip_runtime.h>
#include <hip/hip_bf16.h>
#include <stdint.h>

typedef short bf16x8 __attribute__((ext_vector_type(8)));
typedef short s16x4 __attribute__((ext_vector_type(4)));
typedef float f32x16 __attribute__((ext_vector_type(16)));
typedef float f32x4 __attribute__((ext_vector_type(4)));
typedef unsigned u32x4 __attribute__((ext_vector_type(4)));
typedef unsigned u32x2 __attribute__((ext_vector_type(2)));
typedef int i32x4 __attribute__((ext_vector_type(4)));
typedef short v4i16_t __attribute__((ext_vector_type(4)));
typedef __attribute__((address_space(3))) const char* lds_cptr;
typedef __attribute__((address_space(3))) char* lds_ptr;

#define DEV __device__ __forceinline__
#define SBAR() __builtin_amdgcn_sched_barrier(0)
#define MFMA(a, b, c) __builtin_amdgcn_mfma_f32_32x32x16_bf16(a, b, c, 0, 0, 0)
#define RFL(x) __builtin_amdgcn_readfirstlane(x)

constexpr int UNITB = 8192;
constexpr int GRPB = 8 * UNITB;
constexpr int SCR = 2 * GRPB;
constexpr int LDS_BYTES = SCR + 4096;
constexpr float CEXP = 0.08838834764831845f * 1.4426950408889634f;
constexpr float THR = 8.f;
constexpr int MAXSEG = 62;

DEV int crow(int r, int hi) { return (r & 3) + 8 * (r >> 2) + 4 * hi; }
DEV unsigned cvtpk(float lo, float hi) { unsigned r; asm("v_cvt_pk_bf16_f32 %0, %1, %2" : "=v"(r) : "v"(lo), "v"(hi)); return r; }
DEV float swap_max(float x) {
    auto rr = __builtin_amdgcn_permlane32_swap(__float_as_uint(x), __float_as_uint(x), false, false);
    return __builtin_fmaxf(__uint_as_float(rr[0]), __uint_as_float(rr[1]));
}
DEV float swap_add(float x) {
    auto rr = __builtin_amdgcn_permlane32_swap(__float_as_uint(x), __float_as_uint(x), false, false);
    return __uint_as_float(rr[0]) + __uint_as_float(rr[1]);
}
DEV s16x4 vtr(lds_cptr p) {
    return __builtin_bit_cast(s16x4, __builtin_amdgcn_ds_read_tr16_b64_v4i16((__attribute__((address_space(3))) v4i16_t*)p));
}
DEV void dma_unit(unsigned lds_dst, unsigned voff, __amdgpu_buffer_rsrc_t srd, unsigned soff) {
    asm volatile("s_nop 4\n\ts_mov_b32 m0, %0\n\ts_nop 0\n\tbuffer_load_dwordx4 %2, %3, %4 offen lds\n\t"
                 "s_mov_b32 m0, %1\n\ts_nop 0\n\tbuffer_load_dwordx4 %2, %3, %5 offen lds"
                 :: "s"(lds_dst), "s"(lds_dst + 1024u), "v"(voff), "s"(srd), "s"(soff), "s"(soff + 128u) : "memory");
}

struct Task {
    int head, q0, qend, kfirst, kstride, nunits, kend, nsteps, merge;
};

template <bool GEN>
DEV void run_task(const Task& t, const char* __restrict__ Q, const char* __restrict__ K, const char* __restrict__ V,
                  const int* __restrict__ seg, char* __restrict__ O, int T, int H, char* lds, int w) {
    const int lane = threadIdx.x & 63, r32 = lane & 31, hi = lane >> 5;
    const int g = w >> 2, wq = w & 3;
    const unsigned rowb = (unsigned)H * 256u;
    const unsigned nbytes = (unsigned)T * rowb;
    const __amdgpu_buffer_rsrc_t srdQ = __builtin_amdgcn_make_buffer_rsrc((void*)Q, (short)0, (int)nbytes, 0x00020000);
    const __amdgpu_buffer_rsrc_t srdK = __builtin_amdgcn_make_buffer_rsrc((void*)K, (short)0, (int)nbytes, 0x00020000);
    const __amdgpu_buffer_rsrc_t srdV = __builtin_amdgcn_make_buffer_rsrc((void*)V, (short)0, (int)nbytes, 0x00020000);
    const unsigned lds0 = (unsigned)(uintptr_t)lds;
    const unsigned kdst = lds0 + g * GRPB + wq * 2048, vdst = kdst + 4 * UNITB;
    const unsigned dvoff = (unsigned)(8 * wq + ((lane >> 2) & 7)) * rowb +
                           (unsigned)(4 * (lane >> 5) + ((lane & 3) ^ (2 * (wq & 1) + ((lane >> 4) & 1)))) * 16u;
    const int qrow0 = t.q0 + 32 * wq;
    const bool active = qrow0 < t.qend && t.nunits > 0;
    const unsigned hoff = (unsigned)t.head * 256u;
    auto ksoff = [&](int u) -> unsigned { return u < t.nunits ? (unsigned)(t.kfirst + u * t.kstride) * rowb + hoff : nbytes; };
#define DMA_K(u) dma_unit(kdst + ((u) & 3) * UNITB, dvoff, srdK, ksoff(u))
#define DMA_V(u) dma_unit(vdst + ((u) & 3) * UNITB, dvoff, srdV, ksoff(u))

    bf16x8 qf[8];
    {
        const unsigned qv = (unsigned)(qrow0 + r32) * rowb + hoff + hi * 16u;
        asm volatile("s_nop 4\n\t"
                     "buffer_load_dwordx4 %0, %8, %9, 0 offen\n\t"
                     "buffer_load_dwordx4 %1, %8, %9, 0 offen offset:32\n\t"
                     "buffer_load_dwordx4 %2, %8, %9, 0 offen offset:64\n\t"
                     "buffer_load_dwordx4 %3, %8, %9, 0 offen offset:96\n\t"
                     "buffer_load_dwordx4 %4, %8, %9, 0 offen offset:128\n\t"
                     "buffer_load_dwordx4 %5, %8, %9, 0 offen offset:160\n\t"
                     "buffer_load_dwordx4 %6, %8, %9, 0 offen offset:192\n\t"
                     "buffer_load_dwordx4 %7, %8, %9, 0 offen offset:224"
                     : "=&v"(qf[0]), "=&v"(qf[1]), "=&v"(qf[2]), "=&v"(qf[3]), "=&v"(qf[4]), "=&v"(qf[5]), "=&v"(qf[6]), "=&v"(qf[7])
                     : "v"(qv), "s"(srdQ) : "memory");
    }
    DMA_K(0); DMA_V(0); DMA_K(1); DMA_V(1); DMA_K(2); DMA_V(2); DMA_K(3);

    const lds_cptr gb = (lds_cptr)lds + g * GRPB;
    const lds_cptr kb0 = gb + 2048 * (r32 >> 3) + 64 * (r32 & 7) + 16 * ((0 + hi) ^ ((r32 >> 2) & 3));
    const lds_cptr kb1 = gb + 2048 * (r32 >> 3) + 64 * (r32 & 7) + 16 * ((2 + hi) ^ ((r32 >> 2) & 3));
    const int vq = (lane & 15) >> 2, vp = lane & 3, vblk = (lane >> 4) & 1;
    const lds_cptr vb0 = gb + 4 * UNITB + 64 * (4 * hi + vq) + 16 * ((2 * vblk + (vp >> 1)) ^ (0 + hi)) + 8 * (vp & 1);
    const lds_cptr vb1 = gb + 4 * UNITB + 2048 + 64 * (4 * hi + vq) + 16 * ((2 * vblk + (vp >> 1)) ^ (2 + hi)) + 8 * (vp & 1);

    int sq = 0;
    if (GEN) { const int qr = qrow0 + r32; sq = seg[qr < T ? qr : T - 1]; }

    f32x16 o[4];
#pragma unroll
    for (int d = 0; d < 4; ++d) o[d] = f32x16{};
    float m = -1e30f, l = 0.f;
    f32x16 s;
    bf16x8 kf[8];

#define KREAD(u) do { const int so_ = ((u) & 3) * UNITB; _Pragma("unroll") for (int d0 = 0; d0 < 8; ++d0) \
        kf[d0] = *(const __attribute__((address_space(3))) bf16x8*)(((d0 & 1) ? kb1 : kb0) + so_ + 512 * (d0 >> 1)); } while (0)
#define QK() do { s = MFMA(kf[0], qf[0], f32x16{}); _Pragma("unroll") for (int d0 = 1; d0 < 8; ++d0) s = MFMA(kf[d0], qf[d0], s); } while (0)

    asm volatile("s_waitcnt vmcnt(12)\n\ts_barrier"
                 : "+v"(qf[0]), "+v"(qf[1]), "+v"(qf[2]), "+v"(qf[3]), "+v"(qf[4]), "+v"(qf[5]), "+v"(qf[6]), "+v"(qf[7]) :: "memory");
    SBAR();
    if (active) {
        KREAD(0);
        if (g == 1) { QK(); }
    }
    SBAR();
    asm volatile("s_waitcnt vmcnt(8) lgkmcnt(0)\n\ts_barrier" ::: "memory");
    SBAR();
    if (active && g == 0) { QK(); }

    for (int i = 0; i < t.nsteps; ++i) {
        DMA_V(i + 3); DMA_K(i + 4);
        SBAR();
        if (active && i < t.nunits) {
            const int kbase = t.kfirst + i * t.kstride;
            if (GEN) {
#pragma unroll
                for (int r = 0; r < 16; ++r) {
                    const int key = kbase + crow(r, hi);
                    const int sk = seg[key < T ? key : T - 1];
                    if (key >= t.kend || sk != sq) s[r] = -__builtin_inff();
                }
            } else if (kbase + 32 > t.kend) {
                const int lim = t.kend - kbase - 4 * hi;
#pragma unroll
                for (int r = 0; r < 16; ++r) if ((r & 3) + 8 * (r >> 2) >= lim) s[r] = -__builtin_inff();
            }
            float pm = __builtin_fmaxf(__builtin_fmaxf(s[0], s[1]), s[2]);
#pragma unroll
            for (int r = 3; r < 15; r += 2) pm = __builtin_fmaxf(__builtin_fmaxf(pm, s[r]), s[r + 1]);
            pm = __builtin_fmaxf(pm, s[15]);
            pm = swap_max(pm);
            if (__any((pm - m) * CEXP > THR)) {
                const float mn = __builtin_fmaxf(m, pm);
                const float al = __builtin_amdgcn_exp2f((m - mn) * CEXP);
                m = mn; l *= al;
#pragma unroll
                for (int d = 0; d < 4; ++d)
#pragma unroll
                    for (int r = 0; r < 16; ++r) o[d][r] *= al;
            }
            const float nmc = -m * CEXP;
            float ps = 0.f;
#pragma unroll
            for (int r = 0; r < 16; ++r) { s[r] = __builtin_amdgcn_exp2f(__builtin_fmaf(s[r], CEXP, nmc)); ps += s[r]; }
            l += ps;
            u32x4 p0 = {cvtpk(s[0], s[1]), cvtpk(s[2], s[3]), cvtpk(s[4], s[5]), cvtpk(s[6], s[7])};
            u32x4 p1 = {cvtpk(s[8], s[9]), cvtpk(s[10], s[11]), cvtpk(s[12], s[13]), cvtpk(s[14], s[15])};
            const bf16x8 pf0 = __builtin_bit_cast(bf16x8, p0), pf1 = __builtin_bit_cast(bf16x8, p1);
            const int so = (i & 3) * UNITB;
#pragma unroll
            for (int ks = 0; ks < 2; ++ks)
#pragma unroll
                for (int d = 0; d < 4; ++d) {
                    const s16x4 a = vtr(vb0 + so + 4096 * ks + 512 * d), b = vtr(vb1 + so + 4096 * ks + 512 * d);
                    const bf16x8 vf = {a[0], a[1], a[2], a[3], b[0], b[1], b[2], b[3]};
                    o[d] = MFMA(vf, ks ? pf1 : pf0, o[d]);
                }
            if (i + 1 < t.nunits) KREAD(i + 1);
        }
        SBAR();
        if (g == 0) {
            asm volatile("s_waitcnt vmcnt(8) lgkmcnt(0)\n\ts_barrier" ::: "memory");
            SBAR();
            if (active && i + 1 < t.nunits) { QK(); }
        } else {
            if (active && i + 1 < t.nunits) { QK(); }
            SBAR();
            asm volatile("s_waitcnt vmcnt(8) lgkmcnt(0)\n\ts_barrier" ::: "memory");
        }
        SBAR();
    }
#undef DMA_K
#undef DMA_V
#undef KREAD
#undef QK

    l = swap_add(l);
    float* ml = (float*)(lds + SCR);
    if (hi == 0) { ml[(w * 32 + r32) * 2] = m; ml[(w * 32 + r32) * 2 + 1] = l; }
    asm volatile("s_waitcnt vmcnt(0) lgkmcnt(0)\n\ts_barrier" ::: "memory");
    char* stg = lds + 65536 + w * 4096;
    char* Ow = O + (size_t)hoff;
    auto store_half = [&](const f32x16& a, const f32x16& b, int dpair) {
#pragma unroll
        for (int gq = 0; gq < 4; ++gq) {
            u32x2 x = {cvtpk(a[4 * gq], a[4 * gq + 1]), cvtpk(a[4 * gq + 2], a[4 * gq + 3])};
            u32x2 y = {cvtpk(b[4 * gq], b[4 * gq + 1]), cvtpk(b[4 * gq + 2], b[4 * gq + 3])};
            *(u32x2*)(stg + r32 * 128 + 16 * ((gq) ^ (r32 & 7)) + 8 * hi) = x;
            *(u32x2*)(stg + r32 * 128 + 16 * ((4 + gq) ^ (r32 & 7)) + 8 * hi) = y;
        }
        asm volatile("s_waitcnt lgkmcnt(0)" ::: "memory");
#pragma unroll
        for (int j = 0; j < 4; ++j) {
            const int row = 8 * j + (lane >> 3), c = lane & 7;
            const u32x4 x = *(const u32x4*)(stg + row * 128 + 16 * (c ^ (row & 7)));
            if (qrow0 + row < t.qend) *(u32x4*)(Ow + (size_t)(qrow0 + row) * rowb + dpair * 128 + c * 16) = x;
        }
        asm volatile("s_waitcnt lgkmcnt(0)" ::: "memory");
    };
    if (t.merge) {
        const float mp = ml[((w ^ 4) * 32 + r32) * 2], lp = ml[((w ^ 4) * 32 + r32) * 2 + 1];
        const float ms = __builtin_fmaxf(m, mp);
        const float a = __builtin_amdgcn_exp2f((m - ms) * CEXP), ap = __builtin_amdgcn_exp2f((mp - ms) * CEXP);
        const float sc = a / (a * l + ap * lp);
        f32x4* xb = (f32x4*)(lds + w * 8192);
        const f32x4* xp = (const f32x4*)(lds + (w ^ 4) * 8192);
        f32x16 ka, kb2;
        if (g == 0) {
#pragma unroll
            for (int j = 0; j < 4; ++j) {
                xb[(j) * 64 + lane] = f32x4{o[2][4 * j] * sc, o[2][4 * j + 1] * sc, o[2][4 * j + 2] * sc, o[2][4 * j + 3] * sc};
                xb[(4 + j) * 64 + lane] = f32x4{o[3][4 * j] * sc, o[3][4 * j + 1] * sc, o[3][4 * j + 2] * sc, o[3][4 * j + 3] * sc};
            }
            ka = o[0] * sc; kb2 = o[1] * sc;
        } else {
#pragma unroll
            for (int j = 0; j < 4; ++j) {
                xb[(j) * 64 + lane] = f32x4{o[0][4 * j] * sc, o[0][4 * j + 1] * sc, o[0][4 * j + 2] * sc, o[0][4 * j + 3] * sc};
                xb[(4 + j) * 64 + lane] = f32x4{o[1][4 * j] * sc, o[1][4 * j + 1] * sc, o[1][4 * j + 2] * sc, o[1][4 * j + 3] * sc};
            }
            ka = o[2] * sc; kb2 = o[3] * sc;
        }
        __syncthreads();
#pragma unroll
        for (int j = 0; j < 4; ++j) {
            const f32x4 x = xp[j * 64 + lane], y = xp[(4 + j) * 64 + lane];
#pragma unroll
            for (int e = 0; e < 4; ++e) { ka[4 * j + e] += x[e]; kb2[4 * j + e] += y[e]; }
        }
        store_half(ka, kb2, g);
    } else if (active) {
        const float sc = 1.f / l;
        store_half(o[0] * sc, o[1] * sc, 0);
        store_half(o[2] * sc, o[3] * sc, 1);
    }
    __syncthreads();
}

__global__ __launch_bounds__(512, 2)
void ragged_attn_kernel(const char* __restrict__ Q, const char* __restrict__ K, const char* __restrict__ V,
                        const int* __restrict__ seg, char* __restrict__ O, int T, int H) {
    extern __shared__ __attribute__((aligned(16))) char lds[];
    const int tid = threadIdx.x, lane = tid & 63;
    const int w = RFL(tid >> 6), g = w >> 2;
    int* sc = (int*)(lds + SCR + 2048);
    if (tid < 2) sc[tid] = 0;
    __syncthreads();
    for (int p = tid; p < T; p += 512) {
        const int a = seg[p], b = p > 0 ? seg[p - 1] : a;
        if (p == 0 || a != b) { const int idx = atomicAdd(&sc[0], 1); if (idx < 64) sc[4 + idx] = p; }
        if (a < b) sc[1] = 1;
    }
    __syncthreads();
    const int n = RFL(sc[0]);
    const bool general = n > MAXSEG || RFL(sc[1]) != 0;
    const int G = gridDim.x;
    if (!general) {
        const int mypos = lane < n ? sc[4 + lane] : 0x7fffffff;
        int rank = 0;
        for (int j = 0; j < n; ++j) rank += (__builtin_amdgcn_readlane(mypos, j) < mypos);
        if (lane >= n) rank = lane;
        int start = __builtin_amdgcn_ds_permute(rank * 4, mypos);
        if (lane >= n) start = T;
        int end = __builtin_amdgcn_ds_bpermute(((lane + 1) & 63) * 4, start);
        if (lane >= n) end = T;
        const int len = end - start, nq = (len + 127) >> 7, nk = (len + 31) >> 5;
        int X = 0;
        for (int j = 0; j < n; ++j) {
            const int nkj = __builtin_amdgcn_readlane(nk, j), nqj = __builtin_amdgcn_readlane(nq, j);
            X += nkj > lane ? 2 * nqj : nqj;
        }
        const unsigned long long ok = __ballot(H * ((X + 1) >> 1) <= G);
        const int theta = ok ? __builtin_ctzll(ok) : 0x7fffffff;
        const bool heavy = nk > theta;
        const int val = heavy ? nq : (nq << 16);
        int incl = val;
#pragma unroll
        for (int d = 1; d < 64; d <<= 1) { const int x = __shfl_up(incl, d); if (lane >= d) incl += x; }
        const int tot = __builtin_amdgcn_readlane(incl, 63);
        const int NH = tot & 0xffff, NL = tot >> 16;
        const int ntasks = (NH + ((NL + 1) >> 1)) * H;
        const int exH = (incl - val) & 0xffff, exL = (incl - val) >> 16;
        for (int tau = blockIdx.x; tau < ntasks; tau += G) {
            const int head = tau % H, tix = tau / H;
            Task t;
            t.head = head;
            if (tix < NH) {
                const unsigned long long b = __ballot(heavy && exH <= tix && tix < exH + nq);
                const int sidx = __builtin_ctzll(b);
                const int st = __builtin_amdgcn_readlane(start, sidx), en = __builtin_amdgcn_readlane(end, sidx);
                const int nks = __builtin_amdgcn_readlane(nk, sidx), ex = __builtin_amdgcn_readlane(exH, sidx);
                t.q0 = st + 128 * (tix - ex); t.qend = min(en, t.q0 + 128);
                t.kfirst = st + 32 * g; t.kstride = 64; t.nunits = (nks - g + 1) >> 1; t.kend = en;
                t.nsteps = (nks + 1) >> 1; t.merge = 1;
            } else {
                const int pi = tix - NH;
                const int it0 = pi, it1 = NL - 1 - pi;
                const unsigned long long b0 = __ballot(!heavy && nq > 0 && exL <= it0 && it0 < exL + nq);
                const unsigned long long b1 = __ballot(!heavy && nq > 0 && exL <= it1 && it1 < exL + nq);
                const int s0 = __builtin_ctzll(b0), s1 = __builtin_ctzll(b1);
                const int n0 = __builtin_amdgcn_readlane(nk, s0), n1 = it1 != it0 ? __builtin_amdgcn_readlane(nk, s1) : 0;
                const int sidx = g ? s1 : s0, it = g ? it1 : it0;
                const int st = __builtin_amdgcn_readlane(start, sidx), en = __builtin_amdgcn_readlane(end, sidx);
                const int ex = __builtin_amdgcn_readlane(exL, sidx);
                t.q0 = st + 128 * (it - ex); t.qend = min(en, t.q0 + 128);
                t.kfirst = st; t.kstride = 32; t.nunits = g ? n1 : n0; t.kend = en;
                t.nsteps = max(n0, n1); t.merge = 0;
            }
            run_task<false>(t, Q, K, V, seg, O, T, H, lds, w);
        }
    } else {
        const bool unsorted = RFL(sc[1]) != 0;
        const int ntasks = ((T + 127) >> 7) * H;
        for (int tau = blockIdx.x; tau < ntasks; tau += G) {
            const int head = tau % H, tix = tau / H;
            const int q0 = tix * 128, qend = min(T, q0 + 128);
            int klo = 0, khi = T;
            if (!unsorted) {
                __syncthreads();
                if (tid == 0) { sc[2] = T; sc[3] = 0; }
                __syncthreads();
                const int sa = seg[q0], sb = seg[qend - 1];
                int lo = T, hi2 = 0;
                for (int p = tid; p < T; p += 512) { const int a = seg[p]; if (a == sa) lo = min(lo, p); if (a == sb) hi2 = max(hi2, p + 1); }
                atomicMin(&sc[2], lo); atomicMax(&sc[3], hi2);
                __syncthreads();
                klo = RFL(sc[2]); khi = RFL(sc[3]);
            }
            const int nks = (khi - klo + 31) >> 5;
            Task t;
            t.head = head; t.q0 = q0; t.qend = qend; t.kfirst = klo + 32 * g; t.kstride = 64; t.nunits = (nks - g + 1) >> 1;
            t.kend = khi; t.nsteps = (nks + 1) >> 1; t.merge = 1;
            run_task<true>(t, Q, K, V, seg, O, T, H, lds, w);
        }
    }
}

extern "C" void kernel_launch(void* const* d_in, const int* in_sizes, int n_in, void* d_out, int out_size, void* d_ws, size_t ws_size,
                              hipStream_t stream) {
    const int T = in_sizes[3];
    const int H = in_sizes[0] / (T * 128);
    (void)hipFuncSetAttribute((const void*)ragged_attn_kernel, hipFuncAttributeMaxDynamicSharedMemorySize, LDS_BYTES);
    hipLaunchKernelGGL(ragged_attn_kernel, dim3(256), dim3(512), LDS_BYTES, stream,
                       (const char*)d_in[0], (const char*)d_in[1], (const char*)d_in[2], (const int*)d_in[3], (char*)d_out, T, H);
}
```

```cpp
#include <hip/hip_runtime.h>
#include <hip/hip_bf16.h>
#include <stdint.h>

typedef short bf16x8 __attribute__((ext_vector_type(8)));
typedef short s16x4 __attribute__((ext_vector_type(4)));
typedef float f32x16 __attribute__((ext_vector_type(16)));
typedef float f32x4 __attribute__((ext_vector_type(4)));
typedef float f32x2 __attribute__((ext_vector_type(2)));
typedef __bf16 bf16x2_t __attribute__((ext_vector_type(2)));
typedef unsigned u32x4 __attribute__((ext_vector_type(4)));
typedef unsigned u32x2 __attribute__((ext_vector_type(2)));
typedef int i32x4 __attribute__((ext_vector_type(4)));
typedef short v4i16_t __attribute__((ext_vector_type(4)));
typedef __attribute__((address_space(3))) const char* lds_cptr;

#define DEV __device__ __forceinline__
#define SBAR() __builtin_amdgcn_sched_barrier(0)
#define PIN(x) asm volatile("" : "+v"(x))
#define MFMA(a, b, c) __builtin_amdgcn_mfma_f32_32x32x16_bf16(a, b, c, 0, 0, 0)
#define RFL(x) __builtin_amdgcn_readfirstlane(x)
#define RDL(x, l) __builtin_amdgcn_readlane(x, l)

constexpr int UNITB = 8192;
constexpr int RINGB = 5 * UNITB;
constexpr int GRPB = 2 * RINGB;
constexpr int LDS_BYTES = 2 * GRPB;
constexpr float CEXP = 0.08838834764831845f * 1.4426950408889634f;
#define BIGSUM 65536.f
constexpr int VLEAD = 4;
constexpr int THETA = 8;

DEV int crow(int r, int hi) { return (r & 3) + 8 * (r >> 2) + 4 * hi; }
DEV unsigned cvtpk(float lo, float hi) { f32x2 v = {lo, hi}; return __builtin_bit_cast(unsigned, __builtin_convertvector(v, bf16x2_t)); }
DEV float swap_max(float x) {
    auto rr = __builtin_amdgcn_permlane32_swap(__float_as_uint(x), __float_as_uint(x), false, false);
    return __builtin_fmaxf(__uint_as_float(rr[0]), __uint_as_float(rr[1]));
}
DEV float swap_add(float x) {
    auto rr = __builtin_amdgcn_permlane32_swap(__float_as_uint(x), __float_as_uint(x), false, false);
    return __uint_as_float(rr[0]) + __uint_as_float(rr[1]);
}
DEV s16x4 vtr(lds_cptr p) {
    return __builtin_bit_cast(s16x4, __builtin_amdgcn_ds_read_tr16_b64_v4i16((__attribute__((address_space(3))) v4i16_t*)p));
}
DEV bf16x8 ldsr(lds_cptr p) { return *(const __attribute__((address_space(3))) bf16x8*)p; }
DEV void dma1(unsigned lds_dst, unsigned voff, __amdgpu_buffer_rsrc_t srd, unsigned soff) {
    asm volatile("s_mov_b32 m0, %0\n\ts_nop 3\n\tbuffer_load_dwordx4 %1, %2, %3 offen lds"
                 :: "s"(lds_dst), "v"(voff), "s"(srd), "s"(soff) : "memory");
}
DEV void dma_unit(unsigned lds_dst, unsigned voff, __amdgpu_buffer_rsrc_t srd, unsigned soff) {
    dma1(lds_dst, voff, srd, soff); dma1(lds_dst + 1024u, voff, srd, soff + 128u);
}
template <int CTRL, int RM> DEV int dpp_add(int x) { return x + __builtin_amdgcn_update_dpp(0, x, CTRL, RM, 0xf, false); }
template <int CTRL, int RM> DEV int dpp_min(int x) { return min(x, __builtin_amdgcn_update_dpp(0x7fffffff, x, CTRL, RM, 0xf, false)); }
DEV int scan_add(int x) {
    x = dpp_add<0x111, 0xf>(x); x = dpp_add<0x112, 0xf>(x); x = dpp_add<0x114, 0xf>(x); x = dpp_add<0x118, 0xf>(x);
    x = dpp_add<0x142, 0xa>(x); x = dpp_add<0x143, 0xc>(x); return x;
}
DEV int scan_min(int x) {
    x = dpp_min<0x111, 0xf>(x); x = dpp_min<0x112, 0xf>(x); x = dpp_min<0x114, 0xf>(x); x = dpp_min<0x118, 0xf>(x);
    x = dpp_min<0x142, 0xa>(x); x = dpp_min<0x143, 0xc>(x); return x;
}

#define STAMP(i)
#define CST(i)
#define STAMP_ARG
#define STAMP_PASS

struct Task {
    int head, q0, qend, kfirst, kstride, nunits, kend, nsteps, merge;
};

struct Ctx {
    f32x16 o[4];
    bf16x8 qf[8], kf[8], vf[8];
    float m, l, nmc;
    lds_cptr kb0, kb1, vb0, vb1;
    unsigned kdst, vdst, dvoff, nbytes, ustep, uoff0;
    __amdgpu_buffer_rsrc_t srdK, srdV;
    int n, hi, kfirst, kstride, kend, T, sq;
    const int* seg;
    int s0, s1, s2, s3, s4;
};
DEV unsigned usoff(const Ctx& c, int u) { return u < c.n ? c.uoff0 + (unsigned)u * c.ustep : c.nbytes; }

template <bool GEN> DEV void mask_unit(Ctx& c, f32x16& s, int i) {
    const int kbase = c.kfirst + i * c.kstride;
    if (GEN) {
#pragma unroll
        for (int r = 0; r < 16; ++r) {
            const int key = kbase + crow(r, c.hi);
            const int sk = c.seg[key < c.T ? key : c.T - 1];
            if (key >= c.kend || sk != c.sq) s[r] = -__builtin_inff();
        }
    } else if (kbase + 32 > c.kend) {
        const int lim = c.kend - kbase - 4 * c.hi;
#pragma unroll
        for (int r = 0; r < 16; ++r) if ((r & 3) + 8 * (r >> 2) >= lim) s[r] = -__builtin_inff();
    }
}
DEV float rowmax16(const f32x16& s) {
    float pm = __builtin_fmaxf(__builtin_fmaxf(s[0], s[1]), s[2]);
#pragma unroll
    for (int r = 3; r < 15; r += 2) pm = __builtin_fmaxf(__builtin_fmaxf(pm, s[r]), s[r + 1]);
    return swap_max(__builtin_fmaxf(pm, s[15]));
}

template <bool GEN, bool QKF, bool SMF, bool PVF, bool DMAF>
DEV void step(Ctx& c, f32x16& SC, f32x16& SN, u32x4 (&PP)[2], u32x4 (&PC)[2], int i) {
    const lds_cptr vp0 = c.vb0 + c.s4, vp1 = c.vb1 + c.s4;
    const lds_cptr kp0 = c.kb0 + c.s2, kp1 = c.kb1 + c.s2;
    const unsigned vso = usoff(c, i + 3), kso = usoff(c, i + 6);
    const unsigned vd = c.vdst + c.s3, kd = c.kdst + c.s1;
    if (SMF && (GEN || !QKF)) mask_unit<GEN>(c, SC, i);
    float e[16], ps = 0.f;
    SBAR();
#pragma unroll
    for (int j = 0; j < 16; ++j) {
        if (PVF && j >= VLEAD && j < VLEAD + 8) {
            const int f = j - VLEAD;
            const s16x4 a = vtr(vp0 + 4096 * (f >> 2) + 512 * (f & 3)), b = vtr(vp1 + 4096 * (f >> 2) + 512 * (f & 3));
            c.vf[f] = bf16x8{a[0], a[1], a[2], a[3], b[0], b[1], b[2], b[3]};
        }
        if (j < 8) {
            if (QKF) { SN = MFMA(c.kf[j], c.qf[j], j == 0 ? f32x16{} : SN); }
        } else {
            if (PVF) { c.o[j & 3] = MFMA(c.vf[j - 8], __builtin_bit_cast(bf16x8, PP[(j - 8) >> 2]), c.o[j & 3]); }
        }
        if (QKF && j >= 8 && j < 12) {
            const int d = 2 * (j - 8);
            c.kf[d] = ldsr(kp0 + 512 * (d >> 1)); c.kf[d + 1] = ldsr(kp1 + 512 * (d >> 1));
        }
        if (SMF) {
            e[j] = __builtin_amdgcn_exp2f(__builtin_fmaf(SC[j], CEXP, c.nmc));
            if (j >= 1) { ps += e[j - 1]; PIN(ps); }
            if (j >= 2 && (j & 1) == 0) { PC[(j - 2) >> 3][((j - 2) >> 1) & 3] = cvtpk(e[j - 2], e[j - 1]); PIN(PC[(j - 2) >> 3]); }
            PIN(e[j]);
        }
        if (DMAF) {
            if (j == 2) dma1(vd, c.dvoff, c.srdV, vso);
            if (j == 6) dma1(vd + 1024u, c.dvoff, c.srdV, vso + 128u);
            if (j == 10) dma1(kd, c.dvoff, c.srdK, kso);
            if (j == 14) dma1(kd + 1024u, c.dvoff, c.srdK, kso + 128u);
        }
        SBAR();
    }
    if (SMF) {
        ps += e[15];
        PC[1][3] = cvtpk(e[14], e[15]);
        if (__builtin_expect(__any(!(ps <= BIGSUM)), 0)) {
            const float mn = __builtin_fmaxf(c.m, rowmax16(SC));
            const float al = __builtin_amdgcn_exp2f((c.m - mn) * CEXP);
            c.m = mn; c.l *= al; c.nmc = -mn * CEXP;
#pragma unroll
            for (int d = 0; d < 4; ++d)
#pragma unroll
                for (int r = 0; r < 16; ++r) c.o[d][r] *= al;
            ps = 0.f;
#pragma unroll
            for (int r = 0; r < 16; ++r) { e[r] = __builtin_amdgcn_exp2f(__builtin_fmaf(SC[r], CEXP, c.nmc)); ps += e[r]; }
#pragma unroll
            for (int r = 0; r < 8; ++r) PC[r >> 2][r & 3] = cvtpk(e[2 * r], e[2 * r + 1]);
        }
        c.l += ps;
    }
    { const int t = c.s0; c.s0 = c.s1; c.s1 = c.s2; c.s2 = c.s3; c.s3 = c.s4; c.s4 = t; }
    SBAR();
}
#define WAIT_BAR(N) do { asm volatile("s_waitcnt vmcnt(" #N ") lgkmcnt(0)\n\ts_barrier" ::: "memory"); SBAR(); } while (0)

template <bool GEN>
DEV void run_task(const Task& t, const char* __restrict__ Q, const char* __restrict__ K, const char* __restrict__ V,
                  const int* __restrict__ seg, char* __restrict__ O, int T, int H, char* lds, int w STAMP_ARG) {
    const int g = w >> 2, wq = w & 3;
    const unsigned rowb = (unsigned)H * 256u;
    const unsigned nbytes = (unsigned)T * rowb;
    const __amdgpu_buffer_rsrc_t srdQ = __builtin_amdgcn_make_buffer_rsrc((void*)Q, (short)0, (int)nbytes, 0x00020000);
    Ctx c;
    c.srdK = __builtin_amdgcn_make_buffer_rsrc((void*)K, (short)0, (int)nbytes, 0x00020000);
    c.srdV = __builtin_amdgcn_make_buffer_rsrc((void*)V, (short)0, (int)nbytes, 0x00020000);
    const unsigned lds0 = (unsigned)(uintptr_t)lds;
    c.kdst = lds0 + g * GRPB + wq * 2048; c.vdst = c.kdst + RINGB;
    const int ln = threadIdx.x & 63, lr32 = ln & 31, lhi = ln >> 5;
    c.dvoff = (unsigned)(8 * wq + ((ln >> 2) & 7)) * rowb +
              (unsigned)(4 * (ln >> 5) + ((ln & 3) ^ (2 * (wq & 1) + ((ln >> 4) & 1)))) * 16u;
    const int qrow0 = t.q0 + 32 * wq;
    const int n = t.nunits;
    const bool active = qrow0 < t.qend && n > 0;
    const unsigned hoff = (unsigned)t.head * 256u;
    c.n = n; c.nbytes = nbytes; c.ustep = (unsigned)t.kstride * rowb; c.uoff0 = (unsigned)t.kfirst * rowb + hoff;
    c.hi = lhi; c.kfirst = t.kfirst; c.kstride = t.kstride; c.kend = t.kend; c.T = T; c.seg = seg; c.sq = 0;
    const int qg = t.merge ? 0 : g;
    auto qslot = [&](int b) -> unsigned { return (unsigned)(qg * GRPB + (b == 0 ? 4 * UNITB : RINGB + (b + 1) * UNITB)); };
    {
        const unsigned qso = (unsigned)t.q0 * rowb + hoff;
        if (t.merge) {
            dma_unit(lds0 + qslot(2 * g) + wq * 2048, c.dvoff, srdQ, qso + (unsigned)(2 * g) * 32u * rowb);
            dma_unit(lds0 + qslot(2 * g + 1) + wq * 2048, c.dvoff, srdQ, qso + (unsigned)(2 * g + 1) * 32u * rowb);
        } else {
#pragma unroll
            for (int b = 0; b < 4; ++b) dma_unit(lds0 + qslot(b) + wq * 2048, c.dvoff, srdQ, n > 0 ? qso + (unsigned)b * 32u * rowb : nbytes);
        }
    }
#define DMA_K(u, slot) dma_unit(c.kdst + (slot) * UNITB, c.dvoff, c.srdK, usoff(c, u))
#define DMA_V(u, slot) dma_unit(c.vdst + (slot) * UNITB, c.dvoff, c.srdV, usoff(c, u))
    DMA_K(0, 0); DMA_K(1, 1); DMA_V(0, 0); DMA_K(2, 2); DMA_K(3, 3); DMA_V(1, 1);

    const lds_cptr gb = (lds_cptr)lds + g * GRPB;
    const int ksw0 = 2048 * (lr32 >> 3) + 64 * (lr32 & 7) + 16 * ((0 + lhi) ^ ((lr32 >> 2) & 3));
    const int ksw1 = 2048 * (lr32 >> 3) + 64 * (lr32 & 7) + 16 * ((2 + lhi) ^ ((lr32 >> 2) & 3));
    c.kb0 = gb + ksw0; c.kb1 = gb + ksw1;
    const int vq = (ln & 15) >> 2, vp = ln & 3, vblk = (ln >> 4) & 1;
    c.vb0 = gb + RINGB + 64 * (4 * lhi + vq) + 16 * ((2 * vblk + (vp >> 1)) ^ (0 + lhi)) + 8 * (vp & 1);
    c.vb1 = gb + RINGB + 2048 + 64 * (4 * lhi + vq) + 16 * ((2 * vblk + (vp >> 1)) ^ (2 + lhi)) + 8 * (vp & 1);
    if (GEN) { const int qr = qrow0 + lr32; c.sq = seg[qr < T ? qr : T - 1]; }
#pragma unroll
    for (int d = 0; d < 4; ++d) c.o[d] = f32x16{};
    c.m = -1e30f; c.l = 0.f; c.nmc = 0.f;
    c.s0 = 0; c.s1 = UNITB; c.s2 = 2 * UNITB; c.s3 = 3 * UNITB; c.s4 = 4 * UNITB;
    f32x16 sA, sB;
    u32x4 pA[2], pB[2];
    const int N = t.nsteps;

    WAIT_BAR(8);
    STAMP(3);
    if (active) {
        const lds_cptr qp = (lds_cptr)lds + qslot(wq);
#pragma unroll
        for (int d0 = 0; d0 < 8; ++d0) {
            c.qf[d0] = ldsr(qp + ((d0 & 1) ? ksw1 : ksw0) + 512 * (d0 >> 1));
            c.kf[d0] = ldsr(((d0 & 1) ? c.kb1 : c.kb0) + 512 * (d0 >> 1));
        }
        sA = MFMA(c.kf[0], c.qf[0], f32x16{});
#pragma unroll
        for (int d0 = 1; d0 < 8; ++d0) sA = MFMA(c.kf[d0], c.qf[d0], sA);
#pragma unroll
        for (int d0 = 0; d0 < 8; ++d0) c.kf[d0] = ldsr(((d0 & 1) ? c.kb1 : c.kb0) + UNITB + 512 * (d0 >> 1));
        if (GEN || n == 1) mask_unit<GEN>(c, sA, 0);
        c.m = __builtin_fmaxf(rowmax16(sA), -1e30f);
        c.nmc = -c.m * CEXP;
    }
    SBAR();
    WAIT_BAR(4);
    STAMP(4); CST(24);
    DMA_K(4, 4); DMA_V(2, 2); DMA_K(5, 0); DMA_V(3, 3); DMA_K(6, 1);
    int done;
    if (active) {
        if (n == 1) {
            step<GEN, false, true, false, false>(c, sA, sB, pB, pA, 0);
            WAIT_BAR(12);
        } else {
            step<GEN, true, true, false, false>(c, sA, sB, pB, pA, 0);
            STAMP(16);
            WAIT_BAR(12);
            STAMP(5);
            int i = 1;
            for (;;) {
                if (i + 1 >= n) {
                    sA = sB;
                    pB[0] = pA[0]; pB[1] = pA[1];
                    break;
                }
                step<GEN, true, true, true, true>(c, sB, sA, pA, pB, i);
                WAIT_BAR(12);
                ++i;
                if (i + 1 >= n) break;
                step<GEN, true, true, true, true>(c, sA, sB, pB, pA, i);
                WAIT_BAR(12);
                ++i;
            }
            step<GEN, false, true, true, true>(c, sA, sB, pB, pA, n - 1);
            WAIT_BAR(12);
        }
        step<GEN, false, false, true, false>(c, sA, sB, pA, pB, n);
        done = n;
    } else {
        int i = 0;
        if (n > 0) {
            WAIT_BAR(12);
            for (i = 1; i < n; ++i) {
                dma_unit(c.vdst + c.s4, c.dvoff, c.srdV, usoff(c, i + 3));
                dma_unit(c.kdst + c.s2, c.dvoff, c.srdK, usoff(c, i + 6));
                { const int tt = c.s0; c.s0 = c.s1; c.s1 = c.s2; c.s2 = c.s3; c.s3 = c.s4; c.s4 = tt; }
                WAIT_BAR(12);
            }
        }
        done = n;
    }
    for (int i = done; i < N; ++i) asm volatile("s_barrier" ::: "memory");
#undef DMA_K
#undef DMA_V

    STAMP(12); CST(25);
    float l = swap_add(c.l);
    const float m = c.m;
    asm volatile("s_waitcnt vmcnt(0) lgkmcnt(0)\n\ts_barrier" ::: "memory");
    int le = threadIdx.x & 63;
    asm volatile("" : "+v"(le));
    const int lane = le, r32 = le & 31, hi = le >> 5;
    float* ml = (float*)(lds + 98304);
    char* stg = lds + 65536 + w * 4096;
    char* Ow = O + (size_t)hoff;
    auto store_half = [&](const f32x16& a, const f32x16& b, int dpair) {
#pragma unroll
        for (int gq = 0; gq < 4; ++gq) {
            u32x2 x = {cvtpk(a[4 * gq], a[4 * gq + 1]), cvtpk(a[4 * gq + 2], a[4 * gq + 3])};
            u32x2 y = {cvtpk(b[4 * gq], b[4 * gq + 1]), cvtpk(b[4 * gq + 2], b[4 * gq + 3])};
            *(u32x2*)(stg + r32 * 128 + 16 * ((gq) ^ (r32 & 7)) + 8 * hi) = x;
            *(u32x2*)(stg + r32 * 128 + 16 * ((4 + gq) ^ (r32 & 7)) + 8 * hi) = y;
        }
        asm volatile("s_waitcnt lgkmcnt(0)" ::: "memory");
#pragma unroll
        for (int j = 0; j < 4; ++j) {
            const int row = 8 * j + (lane >> 3), cc = lane & 7;
            const u32x4 x = *(const u32x4*)(stg + row * 128 + 16 * (cc ^ (row & 7)));
            if (qrow0 + row < t.qend) *(u32x4*)(Ow + (size_t)(qrow0 + row) * rowb + dpair * 128 + cc * 16) = x;
        }
        asm volatile("s_waitcnt lgkmcnt(0)" ::: "memory");
    };
    STAMP(13);
    if (t.merge) {
        if (hi == 0) { ml[(w * 32 + r32) * 2] = m; ml[(w * 32 + r32) * 2 + 1] = l; }
        __syncthreads();
        const float mp = ml[((w ^ 4) * 32 + r32) * 2], lp = ml[((w ^ 4) * 32 + r32) * 2 + 1];
        const float ms = __builtin_fmaxf(m, mp);
        const float a = __builtin_amdgcn_exp2f((m - ms) * CEXP), ap = __builtin_amdgcn_exp2f((mp - ms) * CEXP);
        const float sc = a / (a * l + ap * lp);
        f32x4* xb = (f32x4*)(lds + w * 8192);
        const f32x4* xp = (const f32x4*)(lds + (w ^ 4) * 8192);
        f32x16 ka, kb2;
        if (g == 0) {
#pragma unroll
            for (int j = 0; j < 4; ++j) {
                xb[(j) * 64 + lane] = f32x4{c.o[2][4 * j] * sc, c.o[2][4 * j + 1] * sc, c.o[2][4 * j + 2] * sc, c.o[2][4 * j + 3] * sc};
                xb[(4 + j) * 64 + lane] = f32x4{c.o[3][4 * j] * sc, c.o[3][4 * j + 1] * sc, c.o[3][4 * j + 2] * sc, c.o[3][4 * j + 3] * sc};
            }
            ka = c.o[0] * sc; kb2 = c.o[1] * sc;
        } else {
#pragma unroll
            for (int j = 0; j < 4; ++j) {
                xb[(j) * 64 + lane] = f32x4{c.o[0][4 * j] * sc, c.o[0][4 * j + 1] * sc, c.o[0][4 * j + 2] * sc, c.o[0][4 * j + 3] * sc};
                xb[(4 + j) * 64 + lane] = f32x4{c.o[1][4 * j] * sc, c.o[1][4 * j + 1] * sc, c.o[1][4 * j + 2] * sc, c.o[1][4 * j + 3] * sc};
            }
            ka = c.o[2] * sc; kb2 = c.o[3] * sc;
        }
        __syncthreads();
#pragma unroll
        for (int j = 0; j < 4; ++j) {
            const f32x4 x = xp[j * 64 + lane], y = xp[(4 + j) * 64 + lane];
#pragma unroll
            for (int e = 0; e < 4; ++e) { ka[4 * j + e] += x[e]; kb2[4 * j + e] += y[e]; }
        }
        store_half(ka, kb2, g);
    } else if (active) {
        const float sc = 1.f / l;
        store_half(c.o[0] * sc, c.o[1] * sc, 0);
        store_half(c.o[2] * sc, c.o[3] * sc, 1);
    }
    __syncthreads();
}

__global__ __launch_bounds__(512, 2)
void ragged_attn_kernel(const char* __restrict__ Q, const char* __restrict__ K, const char* __restrict__ V,
                        const int* __restrict__ seg, char* __restrict__ O, int T, int H, int hshift
                        ) {
    extern __shared__ __attribute__((aligned(16))) char lds[];
    STAMP(0);
    const int tid = threadIdx.x, lane = tid & 63;
    const int w = RFL(tid >> 6), g = w >> 2;
    int* bl = (int*)lds;
    const int G = gridDim.x;
    const int LPW = (T + 63) >> 6;
    for (int tau = blockIdx.x;; tau += G) {
        int bad = LPW > 64;
        {
            const __amdgpu_buffer_rsrc_t srdS = __builtin_amdgcn_make_buffer_rsrc((void*)seg, (short)0, T * 4, 0x00020000);
            const int base = (w * LPW + lane) * 8;
            const bool on = lane < LPW && base < T;
            const unsigned off = on ? (unsigned)base * 4u : 0x7ffffff0u;
            const u32x4 a = __builtin_amdgcn_raw_buffer_load_b128(srdS, off, 0, 0);
            const u32x4 b = __builtin_amdgcn_raw_buffer_load_b128(srdS, off, 16, 0);
            const int prev = (int)__builtin_amdgcn_raw_buffer_load_b32(srdS, (on && base > 0) ? off - 4u : 0x7ffffff0u, 0, 0);
            const int v[9] = {prev, (int)a[0], (int)a[1], (int)a[2], (int)a[3], (int)b[0], (int)b[1], (int)b[2], (int)b[3]};
            unsigned bits = 0, dec = 0;
#pragma unroll
            for (int j = 0; j < 8; ++j) { bits |= (v[j + 1] != v[j]) ? (1u << j) : 0u; dec |= (v[j + 1] < v[j]) ? (1u << j) : 0u; }
            const int rem = T - base;
            const unsigned vmask = !on ? 0u : (rem >= 8 ? 0xffu : ((1u << rem) - 1u));
            if (base == 0) { bits |= 1u; dec &= ~1u; }
            bits &= vmask; dec &= vmask;
            const bool has = bits != 0;
            const unsigned long long hb = __ballot(has);
            const int rank = __builtin_amdgcn_mbcnt_hi((unsigned)(hb >> 32), __builtin_amdgcn_mbcnt_lo((unsigned)hb, 0));
            bad |= __any(dec != 0 || (bits & (bits - 1)) != 0) || __builtin_popcountll(hb) > 8;
            if (lane < 8) bl[63 - (8 * w + lane)] = T;
            if (has && rank < 8) bl[63 - (8 * w + rank)] = base + __builtin_ctz(bits);
            if (lane == 0) bl[64 + w] = bad;
        }
        __syncthreads();
        const int start = bl[lane];
        const int fl = bl[64 + (lane & 7)];
        asm volatile("s_waitcnt lgkmcnt(0)\n\ts_barrier" ::: "memory");
        STAMP(1);
        if (__any(fl != 0)) break;
        const int sh = __builtin_amdgcn_update_dpp(0x7fffffff, start, 0x138, 0xf, 0xf, false);
        const int end = min(scan_min(sh), T);
        const int len = max(end - start, 0), nq = (len + 127) >> 7, nk = (len + 31) >> 5;
        const bool heavy = nk > THETA;
        const int val = heavy ? nq : (nq << 16);
        const int incl = scan_add(val);
        const int tot = RDL(incl, 63);
        const int NH = tot & 0xffff, NL = tot >> 16;
        const int ntasks = (NH + ((NL + 1) >> 1)) * H;
        if (tau >= ntasks) return;
        const int exH = (incl - val) & 0xffff, exL = (incl - val) >> 16;
        int head, tix;
        if (hshift >= 0) { head = tau & (H - 1); tix = tau >> hshift; } else { head = tau % H; tix = tau / H; }
        Task t;
        t.head = head;
        if (tix < NH) {
            const unsigned long long b = __ballot(heavy && exH <= tix && tix < exH + nq);
            const int sidx = __builtin_ctzll(b);
            const int st = RDL(start, sidx), en = RDL(end, sidx), nks = RDL(nk, sidx), ex = RDL(exH, sidx);
            t.q0 = st + 128 * (tix - ex); t.qend = min(en, t.q0 + 128);
            t.kfirst = st + 32 * g; t.kstride = 64; t.nunits = (nks - g + 1) >> 1; t.kend = en;
            t.nsteps = (nks + 1) >> 1; t.merge = 1;
        } else {
            const int pi = tix - NH;
            const int it0 = pi, it1 = NL - 1 - pi;
            const unsigned long long b0 = __ballot(!heavy && exL <= it0 && it0 < exL + nq);
            const unsigned long long b1 = __ballot(!heavy && exL <= it1 && it1 < exL + nq);
            const int s0 = __builtin_ctzll(b0), s1 = __builtin_ctzll(b1);
            const int n0 = RDL(nk, s0), n1 = it1 != it0 ? RDL(nk, s1) : 0;
            const int sidx = g ? s1 : s0, it = g ? it1 : it0;
            const int st = RDL(start, sidx), en = RDL(end, sidx), ex = RDL(exL, sidx);
            t.q0 = st + 128 * (it - ex); t.qend = min(en, t.q0 + 128);
            t.kfirst = st; t.kstride = 32; t.nunits = g ? n1 : n0; t.kend = en;
            t.nsteps = max(n0, n1); t.merge = 0;
        }
        STAMP(2);
        run_task<false>(t, Q, K, V, seg, O, T, H, lds, w STAMP_PASS);
        if (tau + G >= ntasks) goto finish;
    }
    {
        int* sc = (int*)lds;
        if (tid == 0) sc[0] = 0;
        __syncthreads();
        for (int p = tid + 1; p < T; p += 512) if (seg[p] < seg[p - 1]) sc[0] = 1;
        __syncthreads();
        const bool unsorted = RFL(sc[0]) != 0;
        __syncthreads();
        const int ntasks = ((T + 127) >> 7) * H;
        for (int tau = blockIdx.x; tau < ntasks; tau += G) {
            const int head = tau % H, tix = tau / H;
            const int q0 = tix * 128, qend = min(T, q0 + 128);
            int klo = 0, khi = T;
            if (!unsorted) {
                if (tid == 0) { sc[2] = T; sc[3] = 0; }
                __syncthreads();
                const int sa = seg[q0], sb = seg[qend - 1];
                int lo = T, hi2 = 0;
                for (int p = tid; p < T; p += 512) { const int a = seg[p]; if (a == sa) lo = min(lo, p); if (a == sb) hi2 = max(hi2, p + 1); }
                atomicMin(&sc[2], lo); atomicMax(&sc[3], hi2);
                __syncthreads();
                klo = RFL(sc[2]); khi = RFL(sc[3]);
                __syncthreads();
            }
            const int nks = (khi - klo + 31) >> 5;
            Task t;
            t.head = head; t.q0 = q0; t.qend = qend; t.kfirst = klo + 32 * g; t.kstride = 64; t.nunits = (nks - g + 1) >> 1;
            t.kend = khi; t.nsteps = (nks + 1) >> 1; t.merge = 1;
            run_task<true>(t, Q, K, V, seg, O, T, H, lds, w STAMP_PASS);
        }
    }
finish:;
}

extern "C" void kernel_launch(void* const* d_in, const int* in_sizes, int n_in, void* d_out, int out_size, void* d_ws, size_t ws_size,
                              hipStream_t stream) {
    const int T = in_sizes[3];
    const int H = in_sizes[0] / (T * 128);
    int hshift = -1;
    for (int s = 0; s < 16; ++s) if ((1 << s) == H) hshift = s;
    (void)hipFuncSetAttribute((const void*)ragged_attn_kernel, hipFuncAttributeMaxDynamicSharedMemorySize, LDS_BYTES);
    hipLaunchKernelGGL(ragged_attn_kernel, dim3(256), dim3(512), LDS_BYTES, stream,
                       (const char*)d_in[0], (const char*)d_in[1], (const char*)d_in[2], (const int*)d_in[3], (char*)d_out, T, H, hshift
                       );
}
```

```cpp
#include <hip/hip_runtime.h>
#include <hip/hip_bf16.h>
#include <stdint.h>

typedef short bf16x8 __attribute__((ext_vector_type(8)));
typedef short s16x4 __attribute__((ext_vector_type(4)));
typedef float f32x16 __attribute__((ext_vector_type(16)));
typedef float f32x4 __attribute__((ext_vector_type(4)));
typedef float f32x2 __attribute__((ext_vector_type(2)));
typedef __bf16 bf16x2_t __attribute__((ext_vector_type(2)));
typedef unsigned u32x4 __attribute__((ext_vector_type(4)));
typedef short v4i16_t __attribute__((ext_vector_type(4)));
typedef __attribute__((address_space(3))) const char* lds_cptr;

#define DEV __device__ __forceinline__
#define SBAR() __builtin_amdgcn_sched_barrier(0)
#define PIN(x) asm volatile("" : "+v"(x))
#define MFMA(a, b, c) __builtin_amdgcn_mfma_f32_32x32x16_bf16(a, b, c, 0, 0, 0)
#define RFL(x) __builtin_amdgcn_readfirstlane(x)
#define RDL(x, l) __builtin_amdgcn_readlane(x, l)
DEV int lane_id() { int l; asm volatile("v_mbcnt_lo_u32_b32 %0, -1, 0\n\tv_mbcnt_hi_u32_b32 %0, -1, %0" : "=v"(l)); return l; }

constexpr int UNITB = 8192;
constexpr int RINGB = 5 * UNITB;
constexpr int GRPB = 2 * RINGB;
constexpr int LDS_BYTES = 2 * GRPB;
constexpr float CEXP = 0.08838834764831845f * 1.4426950408889634f;
#define BIGSUM 65536.f
#define VLEAD 4
constexpr int THETA = 8;

DEV int crow(int r, int hi) { return (r & 3) + 8 * (r >> 2) + 4 * hi; }
DEV unsigned cvtpk(float lo, float hi) { f32x2 v = {lo, hi}; return __builtin_bit_cast(unsigned, __builtin_convertvector(v, bf16x2_t)); }
DEV float swap_max(float x) {
    auto rr = __builtin_amdgcn_permlane32_swap(__float_as_uint(x), __float_as_uint(x), false, false);
    return __builtin_fmaxf(__uint_as_float(rr[0]), __uint_as_float(rr[1]));
}
DEV float swap_add(float x) {
    auto rr = __builtin_amdgcn_permlane32_swap(__float_as_uint(x), __float_as_uint(x), false, false);
    return __uint_as_float(rr[0]) + __uint_as_float(rr[1]);
}
DEV s16x4 vtr(lds_cptr p) {
    return __builtin_bit_cast(s16x4, __builtin_amdgcn_ds_read_tr16_b64_v4i16((__attribute__((address_space(3))) v4i16_t*)p));
}
DEV bf16x8 ldsr(lds_cptr p) { return *(const __attribute__((address_space(3))) bf16x8*)p; }
#define LD_AUX ""
#define PF_AUX ""
#define DMA_NOP "0"
template <int IMM> DEV void dmaA(unsigned lds_base, unsigned voff, __amdgpu_buffer_rsrc_t srd, unsigned soff) {
    asm volatile("s_add_u32 m0, %0, %4\n\ts_nop " DMA_NOP "\n\tbuffer_load_dwordx4 %1, %2, %3 offen" LD_AUX " lds"
                 :: "s"(lds_base), "v"(voff), "s"(srd), "s"(soff), "i"(IMM) : "memory", "scc");
}
template <int IMM> DEV void dmaB(unsigned lds_base, unsigned voff, __amdgpu_buffer_rsrc_t srd, unsigned soff) {
    asm volatile("s_add_u32 m0, %0, %4\n\ts_nop " DMA_NOP "\n\tbuffer_load_dwordx4 %1, %2, %3 offen offset:128" LD_AUX " lds"
                 :: "s"(lds_base), "v"(voff), "s"(srd), "s"(soff), "i"(IMM + 896) : "memory", "scc");
}
template <int IMM> DEV void dma_unit(unsigned lds_base, unsigned voff, __amdgpu_buffer_rsrc_t srd, unsigned soff) {
    dmaA<IMM>(lds_base, voff, srd, soff); dmaB<IMM>(lds_base, voff, srd, soff);
}
template <int CTRL, int RM> DEV int dpp_add(int x) { return x + __builtin_amdgcn_update_dpp(0, x, CTRL, RM, 0xf, false); }
template <int CTRL, int RM> DEV int dpp_min(int x) { return min(x, __builtin_amdgcn_update_dpp(0x7fffffff, x, CTRL, RM, 0xf, false)); }
DEV int scan_add(int x) {
    x = dpp_add<0x111, 0xf>(x); x = dpp_add<0x112, 0xf>(x); x = dpp_add<0x114, 0xf>(x); x = dpp_add<0x118, 0xf>(x);
    x = dpp_add<0x142, 0xa>(x); x = dpp_add<0x143, 0xc>(x); return x;
}
DEV int scan_min(int x) {
    x = dpp_min<0x111, 0xf>(x); x = dpp_min<0x112, 0xf>(x); x = dpp_min<0x114, 0xf>(x); x = dpp_min<0x118, 0xf>(x);
    x = dpp_min<0x142, 0xa>(x); x = dpp_min<0x143, 0xc>(x); return x;
}

struct Task {
    int head, q0, qend, kfirst, kstride, nunits, kend, nsteps, merge, nother;
};

struct LaneK { unsigned dvoff; lds_cptr kb0, kb1, vb0, vb1; };
struct Ctx {
    f32x16 o[4];
    bf16x8 qf[8], kf[8], vf[8];
    float m, l, nmc;
    lds_cptr kb0, kb1, vb0, vb1;
    unsigned kdst, dvoff, nbytes, ustep, uoff0;
    __amdgpu_buffer_rsrc_t srdK, srdV;
    int n, hi, kfirst, kstride, kend, T, sq;
    const int* seg;
    int s0, s1, s2, s3, s4;
};
DEV unsigned usoff(const Ctx& c, int u) { return u < c.n ? c.uoff0 + (unsigned)u * c.ustep : c.nbytes; }

template <bool GEN> DEV void mask_unit(Ctx& c, f32x16& s, int i) {
    const int kbase = c.kfirst + i * c.kstride;
    if (GEN) {
        float neg = -__builtin_inff();
        asm volatile("" : "+v"(neg));
#pragma unroll
        for (int r = 0; r < 16; ++r) {
            const int key = kbase + crow(r, c.hi);
            const int sk = c.seg[key < c.T ? key : c.T - 1];
            s[r] = (key >= c.kend || sk != c.sq) ? neg : s[r];
        }
    } else if (kbase + 32 > c.kend) {
        int lim = c.kend - kbase - 4 * (lane_id() >> 5);
        float neg = -__builtin_inff();
        asm volatile("" : "+v"(neg), "+v"(lim));
#pragma unroll
        for (int r = 0; r < 16; ++r) { s[r] = (r & 3) + 8 * (r >> 2) >= lim ? neg : s[r]; }
    }
}
DEV float rowmax16(const f32x16& s) {
    float pm = __builtin_fmaxf(__builtin_fmaxf(s[0], s[1]), s[2]);
#pragma unroll
    for (int r = 3; r < 15; r += 2) pm = __builtin_fmaxf(__builtin_fmaxf(pm, s[r]), s[r + 1]);
    return swap_max(__builtin_fmaxf(pm, s[15]));
}

template <int DM, int Q> DEV void dma_slot(Ctx& c, int i) {
    if (DM == 1) {
        if (Q == 0) dmaA<0>(c.kdst + c.s0, c.dvoff, c.srdK, usoff(c, i + 5));
        if (Q == 1) dmaB<0>(c.kdst + c.s0, c.dvoff, c.srdK, usoff(c, i + 5));
        if (Q == 2) dmaA<RINGB>(c.kdst + c.s2, c.dvoff, c.srdV, usoff(c, i + 2));
        if (Q == 3) dmaB<RINGB>(c.kdst + c.s2, c.dvoff, c.srdV, usoff(c, i + 2));
    } else if (DM == 2) {
        if (Q == 0) dmaA<RINGB + 1 * UNITB>(c.kdst, c.dvoff, c.srdV, usoff(c, 1));
        if (Q == 1) dmaB<RINGB + 1 * UNITB>(c.kdst, c.dvoff, c.srdV, usoff(c, 1));
        if (Q == 2) dmaA<4 * UNITB>(c.kdst, c.dvoff, c.srdK, usoff(c, 4));
        if (Q == 3) dmaB<4 * UNITB>(c.kdst, c.dvoff, c.srdK, usoff(c, 4));
        if (Q == 4) dmaA<0>(c.kdst, c.dvoff, c.srdK, usoff(c, 5));
        if (Q == 5) dmaB<0>(c.kdst, c.dvoff, c.srdK, usoff(c, 5));
    } else if (DM == 3) {
        if (Q == 0) dmaA<RINGB + 2 * UNITB>(c.kdst, c.dvoff, c.srdV, usoff(c, 2));
        if (Q == 1) dmaB<RINGB + 2 * UNITB>(c.kdst, c.dvoff, c.srdV, usoff(c, 2));
        if (Q == 2) dmaA<RINGB + 3 * UNITB>(c.kdst, c.dvoff, c.srdV, usoff(c, 3));
        if (Q == 3) dmaB<RINGB + 3 * UNITB>(c.kdst, c.dvoff, c.srdV, usoff(c, 3));
        if (Q == 4) dmaA<1 * UNITB>(c.kdst, c.dvoff, c.srdK, usoff(c, 6));
        if (Q == 5) dmaB<1 * UNITB>(c.kdst, c.dvoff, c.srdK, usoff(c, 6));
    }
}
DEV void rot_slots(Ctx& c) { const int t = c.s0; c.s0 = c.s1; c.s1 = c.s2; c.s2 = c.s3; c.s3 = c.s4; c.s4 = t; }

template <bool GEN, bool QKF, bool SMF, bool PVF, int DM>
DEV void step(Ctx& c, f32x16& SC, f32x16& SN, u32x4 (&PP)[2], u32x4 (&PC)[2], int i) {
    const lds_cptr vp0 = c.vb0 + c.s4, vp1 = c.vb1 + c.s4;
    const lds_cptr kp0 = c.kb0 + c.s2, kp1 = c.kb1 + c.s2;
    if (SMF && GEN) mask_unit<GEN>(c, SC, i);
    float e[16], ps = 0.f;
    unsigned pk[8];
    SBAR();
#pragma unroll
    for (int j = 0; j < 16; ++j) {
        if (PVF && j >= VLEAD && j < VLEAD + 8) {
            const int f = j - VLEAD;
            const s16x4 a = vtr(vp0 + 4096 * (f >> 2) + 512 * (f & 3)), b = vtr(vp1 + 4096 * (f >> 2) + 512 * (f & 3));
            c.vf[f] = bf16x8{a[0], a[1], a[2], a[3], b[0], b[1], b[2], b[3]};
        }
        if (DM == 2) {
            if (j < 4) {
                c.vf[2 * j] = ldsr(c.kb0 + UNITB + 512 * j); c.vf[2 * j + 1] = ldsr(c.kb1 + UNITB + 512 * j);
            } else if (j < 12) SN = MFMA(c.vf[j - 4], c.qf[j - 4], j == 4 ? f32x16{} : SN);
        } else if (j < 8) {
            if (QKF) { SN = MFMA(c.kf[j], c.qf[j], j == 0 ? f32x16{} : SN); }
        } else {
            if (PVF) { c.o[j & 3] = MFMA(c.vf[j - 8], __builtin_bit_cast(bf16x8, PP[(j - 8) >> 2]), (DM == 3 && j < 12) ? f32x16{} : c.o[j & 3]); }
        }
        if (QKF && j >= 8 && j < 12) {
            const int d = 2 * (j - 8);
            c.kf[d] = ldsr(kp0 + 512 * (d >> 1)); c.kf[d + 1] = ldsr(kp1 + 512 * (d >> 1));
        }
        if (SMF) {
            e[j] = __builtin_amdgcn_exp2f(__builtin_fmaf(SC[j], CEXP, c.nmc));
            if (j >= 1) { ps += e[j - 1]; PIN(ps); }
            if (j >= 2 && (j & 1) == 0) { pk[(j - 2) >> 1] = cvtpk(e[j - 2], e[j - 1]); PIN(pk[(j - 2) >> 1]); }
            PIN(e[j]);
        }
        if (DM == 1) {
            if (j == 0) dma_slot<DM, 0>(c, i); if (j == 1) dma_slot<DM, 1>(c, i); if (j == 14) dma_slot<DM, 2>(c, i); if (j == 15) dma_slot<DM, 3>(c, i);
        } else if (DM >= 2) {
            if (j == 0) dma_slot<DM, 0>(c, i); if (j == 2) dma_slot<DM, 1>(c, i); if (j == 4) dma_slot<DM, 2>(c, i);
            if (j == 12) dma_slot<DM, 3>(c, i); if (j == 14) dma_slot<DM, 4>(c, i); if (j == 15) dma_slot<DM, 5>(c, i);
        }
        SBAR();
    }
    if (SMF) {
        ps += e[15];
        pk[7] = cvtpk(e[14], e[15]);
        if (__builtin_expect(__any(!(ps <= BIGSUM)), 0)) {
            const float mn = __builtin_fmaxf(c.m, rowmax16(SC));
            const float al = __builtin_amdgcn_exp2f((c.m - mn) * CEXP);
            c.m = mn; c.l *= al; c.nmc = -mn * CEXP;
            if (PVF) {
#pragma unroll
                for (int d = 0; d < 4; ++d)
#pragma unroll
                    for (int r = 0; r < 16; ++r) c.o[d][r] *= al;
            }
            ps = 0.f;
#pragma unroll
            for (int r = 0; r < 8; ++r) {
                const float x = __builtin_amdgcn_exp2f(__builtin_fmaf(SC[2 * r], CEXP, c.nmc)), y = __builtin_amdgcn_exp2f(__builtin_fmaf(SC[2 * r + 1], CEXP, c.nmc));
                ps += x + y; pk[r] = cvtpk(x, y);
            }
        }
        c.l += ps;
        PC[0] = u32x4{pk[0], pk[1], pk[2], pk[3]}; PC[1] = u32x4{pk[4], pk[5], pk[6], pk[7]};
    }
    rot_slots(c);
    SBAR();
}
#define WAIT_BAR(N) do { asm volatile("s_waitcnt vmcnt(" #N ") lgkmcnt(0)\n\ts_barrier" ::: "memory"); SBAR(); } while (0)

template <bool GEN>
DEV void run_task(const Task& t, const char* __restrict__ Q, const char* __restrict__ K, const char* __restrict__ V,
                  const int* __restrict__ seg, char* __restrict__ O, int T, int H, char* lds, int w, const LaneK& lk) {
    const int g = w >> 2, wq = w & 3;
    const unsigned rowb = (unsigned)H * 256u;
    const unsigned nbytes = (unsigned)T * rowb;
    const __amdgpu_buffer_rsrc_t srdQ = __builtin_amdgcn_make_buffer_rsrc((void*)Q, (short)0, (int)nbytes, 0x00020000);
    Ctx c;
    c.srdK = __builtin_amdgcn_make_buffer_rsrc((void*)K, (short)0, (int)((unsigned)t.kend * rowb), 0x00020000);
    c.srdV = __builtin_amdgcn_make_buffer_rsrc((void*)V, (short)0, (int)((unsigned)t.kend * rowb), 0x00020000);
    const unsigned lds0 = (unsigned)(uintptr_t)lds;
    c.kdst = lds0 + g * GRPB + wq * 2048;
    c.dvoff = lk.dvoff;
    const int qrow0 = t.q0 + 32 * wq;
    const int n = t.nunits;
    const bool active = qrow0 < t.qend && n > 0;
    const unsigned hoff = (unsigned)t.head * 256u;
    c.n = n; c.nbytes = nbytes; c.ustep = (unsigned)t.kstride * rowb; c.uoff0 = (unsigned)t.kfirst * rowb + hoff;
    c.hi = GEN ? lane_id() >> 5 : 0; c.kfirst = t.kfirst; c.kstride = t.kstride; c.kend = t.kend; c.T = T; c.seg = seg; c.sq = 0;
    const int qg = t.merge ? 0 : g;
    {
        const unsigned qso = (unsigned)t.q0 * rowb + hoff, qb = lds0 + wq * 2048, qstep = 32u * rowb;
        if (t.merge) {
            if (g == 0) { dma_unit<4 * UNITB>(qb, c.dvoff, srdQ, qso); dma_unit<RINGB + 2 * UNITB>(qb, c.dvoff, srdQ, qso + qstep); }
            else { dma_unit<RINGB + 3 * UNITB>(qb, c.dvoff, srdQ, qso + 2 * qstep); dma_unit<RINGB + 4 * UNITB>(qb, c.dvoff, srdQ, qso + 3 * qstep); }
        } else {
            const unsigned q0o = n > 0 ? qso : nbytes;
            dma_unit<4 * UNITB>(c.kdst, c.dvoff, srdQ, q0o); dma_unit<RINGB + 2 * UNITB>(c.kdst, c.dvoff, srdQ, q0o + qstep);
            dma_unit<RINGB + 3 * UNITB>(c.kdst, c.dvoff, srdQ, q0o + 2 * qstep); dma_unit<RINGB + 4 * UNITB>(c.kdst, c.dvoff, srdQ, q0o + 3 * qstep);
        }
    }
#define DMA_K(u, slot) dma_unit<(slot) * UNITB>(c.kdst, c.dvoff, c.srdK, usoff(c, u))
#define DMA_V(u, slot) dma_unit<RINGB + (slot) * UNITB>(c.kdst, c.dvoff, c.srdV, usoff(c, u))
    DMA_K(0, 0); DMA_K(1, 1); DMA_K(2, 2);

    c.kb0 = lk.kb0; c.kb1 = lk.kb1; c.vb0 = lk.vb0; c.vb1 = lk.vb1;
    if (GEN) { const int qr = qrow0 + (lane_id() & 31); c.sq = seg[qr < T ? qr : T - 1]; }
    c.m = -1e30f; c.l = 0.f; c.nmc = 0.f;
    c.s0 = 0; c.s1 = UNITB; c.s2 = 2 * UNITB; c.s3 = 3 * UNITB; c.s4 = 4 * UNITB;
    f32x16 sA, sB;
    u32x4 pA[2], pB[2];
    const int N = t.nsteps;
    SBAR();
    WAIT_BAR(4);
    if (active) {
        const int qd = (qg - g) * GRPB + (wq == 0 ? 4 * UNITB : RINGB + (wq + 1) * UNITB);
#pragma unroll
        for (int d0 = 0; d0 < 8; ++d0) {
            c.kf[d0] = ldsr(((d0 & 1) ? c.kb1 : c.kb0) + 512 * (d0 >> 1));
            c.qf[d0] = ldsr(((d0 & 1) ? c.kb1 : c.kb0) + qd + 512 * (d0 >> 1));
        }
        SBAR();
#pragma unroll
        for (int d0 = 0; d0 < 8; ++d0) {
            sA = MFMA(c.kf[d0], c.qf[d0], d0 == 0 ? f32x16{} : sA);
            if (d0 == 1) dmaA<3 * UNITB>(c.kdst, c.dvoff, c.srdK, usoff(c, 3));
            if (d0 == 3) dmaB<3 * UNITB>(c.kdst, c.dvoff, c.srdK, usoff(c, 3));
            if (d0 == 5) dmaA<RINGB>(c.kdst, c.dvoff, c.srdV, usoff(c, 0));
            if (d0 == 7) dmaB<RINGB>(c.kdst, c.dvoff, c.srdV, usoff(c, 0));
            SBAR();
        }
        if (GEN || n == 1) mask_unit<GEN>(c, sA, 0);
        c.m = __builtin_fmaxf(rowmax16(sA), -1e30f);
        c.nmc = -c.m * CEXP;
    } else {
        DMA_K(3, 3); DMA_V(0, 0);
    }
    SBAR();
    WAIT_BAR(4);
    float m, l;
#define PUBLISH_ML() do { l = swap_add(c.l); m = c.m; ((f32x2*)(lds + g * GRPB + RINGB + ((n + 1) % 5) * UNITB))[wq * 64 + lane_id()] = f32x2{m, l}; } while (0)
    if (!active || n <= 2) {
#pragma unroll
        for (int d = 0; d < 4; ++d) c.o[d] = f32x16{};
    }
    if (active) {
        if (n == 1) {
            step<GEN, false, true, false, 0>(c, sA, sB, pB, pA, 0);
            PUBLISH_ML();
            WAIT_BAR(0);
        } else {
            step<GEN, true, true, false, 2>(c, sA, sB, pB, pA, 0);
            WAIT_BAR(6);
            if (n > 2) {
                step<GEN, true, true, true, 3>(c, sB, sA, pA, pB, 1);
                WAIT_BAR(8);
                int i = 2;
                for (; i + 2 < n; i += 2) {
                    step<GEN, true, true, true, 1>(c, sA, sB, pB, pA, i);
                    WAIT_BAR(8);
                    step<GEN, true, true, true, 1>(c, sB, sA, pA, pB, i + 1);
                    WAIT_BAR(8);
                }
                if (i + 1 < n) {
                    step<GEN, true, true, true, 1>(c, sA, sB, pB, pA, i);
                    WAIT_BAR(8);
                    sA = sB; pB[0] = pA[0]; pB[1] = pA[1];
                }
            } else { sA = sB; pB[0] = pA[0]; pB[1] = pA[1]; }
            const int lim = t.kend - (t.kfirst + (n - 1) * t.kstride);
            const bool exact = !GEN && lim < 32 && __any(c.m < 0.f);
            if (exact) mask_unit<false>(c, sA, n - 1);
            step<GEN, false, true, true, 0>(c, sA, sB, pB, pA, n - 1);
            if (!GEN && lim < 32 && !exact) {
                const int L = max(lim - 4 * (lane_id() >> 5), 0);
                c.l -= (float)(16 - (4 * (L >> 3) + min(L & 7, 4))) * __builtin_amdgcn_exp2f(c.nmc);
            }
            PUBLISH_ML();
            WAIT_BAR(0);
        }
    } else if (n > 0) {
        if (n > 1) {
            DMA_V(1, 1); DMA_K(4, 4); DMA_K(5, 0);
            WAIT_BAR(6);
            rot_slots(c);
            if (n > 2) {
                DMA_V(2, 2); DMA_V(3, 3); DMA_K(6, 1);
                WAIT_BAR(8);
                rot_slots(c);
                for (int i = 2; i + 1 < n; ++i) {
                    dma_unit<0>(c.kdst + c.s0, c.dvoff, c.srdK, usoff(c, i + 5));
                    dma_unit<RINGB>(c.kdst + c.s2, c.dvoff, c.srdV, usoff(c, i + 2));
                    rot_slots(c);
                    WAIT_BAR(8);
                }
            }
        }
        PUBLISH_ML();
        WAIT_BAR(0);
    } else {
        PUBLISH_ML();
        asm volatile("s_waitcnt vmcnt(0) lgkmcnt(0)" ::: "memory");
    }
#undef DMA_K
#undef DMA_V
    const int lane = lane_id(), r32 = lane & 31, hi = lane >> 5;
    const __amdgpu_buffer_rsrc_t srdO = __builtin_amdgcn_make_buffer_rsrc((void*)O, (short)0, (int)((unsigned)t.qend * rowb), 0x00020000);
    const unsigned ovoff = (unsigned)(qrow0 + (lane >> 3)) * rowb + hoff + (lane & 7) * 16;
    auto store_half = [&](const f32x16& a, const f32x16& b, int dpair, char* stg) {
#pragma unroll
        for (int blk = 0; blk < 2; ++blk) {
            const f32x16& x = blk ? b : a;
#pragma unroll
            for (int k = 0; k < 4; k += 2) {
                const unsigned ax = cvtpk(x[4 * k], x[4 * k + 1]), ay = cvtpk(x[4 * k + 2], x[4 * k + 3]);
                const unsigned bx = cvtpk(x[4 * k + 4], x[4 * k + 5]), by = cvtpk(x[4 * k + 6], x[4 * k + 7]);
                const auto r0 = __builtin_amdgcn_permlane32_swap(ax, bx, false, false);
                const auto r1 = __builtin_amdgcn_permlane32_swap(ay, by, false, false);
                *(u32x4*)(stg + r32 * 128 + 16 * ((4 * blk + k + hi) ^ (r32 & 7))) = u32x4{r0[0], r1[0], r0[1], r1[1]};
            }
        }
        asm volatile("s_waitcnt lgkmcnt(0)" ::: "memory");
        u32x4 y[4];
#pragma unroll
        for (int j = 0; j < 4; ++j) y[j] = *(const u32x4*)(stg + (8 * j + (lane >> 3)) * 128 + 16 * ((lane & 7) ^ ((lane >> 3) & 7)));
#define ST_AUX 16
#pragma unroll
        for (int j = 0; j < 4; ++j)
            __builtin_amdgcn_raw_buffer_store_b128(y[j], srdO, ovoff + (unsigned)(8 * j) * rowb + dpair * 128, 0, ST_AUX);
    };
    const lds_cptr vp0 = c.vb0 + c.s4, vp1 = c.vb1 + c.s4;
    auto pv = [&](int ks, int d) {
        const s16x4 x = vtr(vp0 + 4096 * ks + 512 * d), y = vtr(vp1 + 4096 * ks + 512 * d);
        return bf16x8{x[0], x[1], x[2], x[3], y[0], y[1], y[2], y[3]};
    };
    if (t.merge) {
        f32x4* xb = (f32x4*)(lds + g * GRPB + wq * 8192);
        const f32x4* xp = (const f32x4*)(lds + (g ^ 1) * GRPB + wq * 8192);
        const f32x2* mlp_p = (const f32x2*)(lds + (g ^ 1) * GRPB + RINGB + ((t.nother + 1) % 5) * UNITB) + wq * 64 + lane;
        auto put = [&](const f32x16& x, int half) {
#pragma unroll
            for (int j = 0; j < 4; ++j) xb[(4 * half + j) * 64 + lane] = f32x4{x[4 * j], x[4 * j + 1], x[4 * j + 2], x[4 * j + 3]};
        };
        auto merge = [&](f32x16& k0, f32x16& k1, f32x16& g0, f32x16& g1, int kd, int gd) {
            if (!GEN || active) {
                bf16x8 vf[8];
#pragma unroll
                for (int f = 0; f < 4; ++f) vf[f] = pv(f >> 1, gd + (f & 1));
#pragma unroll
                for (int f = 0; f < 4; ++f) vf[4 + f] = pv(f >> 1, kd + (f & 1));
                g0 = MFMA(vf[0], __builtin_bit_cast(bf16x8, pA[0]), g0); g1 = MFMA(vf[1], __builtin_bit_cast(bf16x8, pA[0]), g1);
                g0 = MFMA(vf[2], __builtin_bit_cast(bf16x8, pA[1]), g0); g1 = MFMA(vf[3], __builtin_bit_cast(bf16x8, pA[1]), g1);
                SBAR();
                k0 = MFMA(vf[4], __builtin_bit_cast(bf16x8, pA[0]), k0); k1 = MFMA(vf[5], __builtin_bit_cast(bf16x8, pA[0]), k1);
                SBAR();
                put(g0, 0);
                SBAR();
                k0 = MFMA(vf[6], __builtin_bit_cast(bf16x8, pA[1]), k0); k1 = MFMA(vf[7], __builtin_bit_cast(bf16x8, pA[1]), k1);
                SBAR();
                put(g1, 1);
            } else { put(g0, 0); put(g1, 1); }
            for (int i = n; i < N; ++i) asm volatile("s_barrier" ::: "memory");
            const f32x2 mlp = *mlp_p;
            const float ms = __builtin_fmaxf(m, mlp[0]);
            const float a = __builtin_amdgcn_exp2f((m - ms) * CEXP), ap = __builtin_amdgcn_exp2f((mlp[0] - ms) * CEXP);
            const float inv = __builtin_amdgcn_rcpf(a * l + ap * mlp[1]);
            const float sa = a * inv, sp = ap * inv;
            k0 *= sa; k1 *= sa;
            PIN(k0); PIN(k1);
            asm volatile("s_waitcnt lgkmcnt(0)\n\ts_barrier" ::: "memory");
            f32x4 xr[8];
#pragma unroll
            for (int j = 0; j < 8; ++j) xr[j] = xp[j * 64 + lane];
#pragma unroll
            for (int j = 0; j < 4; ++j)
#pragma unroll
                for (int e = 0; e < 4; ++e) {
                    k0[4 * j + e] = __builtin_fmaf(xr[j][e], sp, k0[4 * j + e]);
                    k1[4 * j + e] = __builtin_fmaf(xr[4 + j][e], sp, k1[4 * j + e]);
                }
            store_half(k0, k1, kd >> 1, lds + g * GRPB + RINGB + wq * 4096);
        };
        if (!GEN && !active) {
            for (int i = n; i <= N; ++i) asm volatile("s_barrier" ::: "memory");
        } else
        if (g == 0) merge(c.o[0], c.o[1], c.o[2], c.o[3], 0, 2);
        else        merge(c.o[2], c.o[3], c.o[0], c.o[1], 2, 0);
    } else {
        if (active) {
#pragma unroll
            for (int f = 0; f < 8; ++f) c.o[f & 3] = MFMA(pv(f >> 2, f & 3), __builtin_bit_cast(bf16x8, pA[f >> 2]), c.o[f & 3]);
            const float sc = __builtin_amdgcn_rcpf(l);
            store_half(c.o[0] * sc, c.o[1] * sc, 0, lds + g * GRPB + wq * 8192);
            store_half(c.o[2] * sc, c.o[3] * sc, 1, lds + g * GRPB + wq * 8192 + 4096);
        }
        for (int i = n; i < N; ++i) asm volatile("s_barrier" ::: "memory");
    }
}

__global__ __launch_bounds__(512, 2)
void ragged_attn_kernel(const char* __restrict__ Q, const char* __restrict__ K, const char* __restrict__ V,
                        const int* __restrict__ seg, char* __restrict__ O, int T, int H, int hshift
                        ) {
    extern __shared__ __attribute__((aligned(16))) char lds[];
    asm volatile("" :: "s"(Q), "s"(K), "s"(V), "s"(O), "s"(seg), "s"(T), "s"(H), "s"(hshift));
    const int tid = threadIdx.x, lane0 = tid & 63;
    const int w = RFL(tid >> 6), g = w >> 2;
    int* bl = (int*)(lds + RINGB + UNITB);
    const int G = gridDim.x;
    const int LPW = (T + 63) >> 6;
    const __amdgpu_buffer_rsrc_t srdS = __builtin_amdgcn_make_buffer_rsrc((void*)seg, (short)0, T * 4, 0x00020000);
    u32x4 sa, sb; unsigned sp;
#define ISSUE_SEG(lane) { const int sadj = max(min((w * LPW + (lane)) * 8, T - 8), 0); asm volatile("s_nop 4\n\tbuffer_load_dwordx4 %0, %3, %5, 0 offen\n\tbuffer_load_dwordx4 %1, %3, %5, 0 offen offset:16\n\tbuffer_load_dword %2, %4, %5, 0 offen" \
        : "=&v"(sa), "=&v"(sb), "=&v"(sp) : "v"((unsigned)sadj * 4u), "v"(sadj > 0 ? (unsigned)sadj * 4u - 4u : 0u), "s"(srdS) : "memory"); }
    ISSUE_SEG(lane0);
    {
        const int x = blockIdx.x & 7, j = blockIdx.x >> 3;
        const int TP = (T + 31) >> 5;
        const int ls = hshift - 2;
        const __amdgpu_buffer_rsrc_t srdQ = __builtin_amdgcn_make_buffer_rsrc((void*)Q, (short)0, T * H * 256, 0x00020000);
        const unsigned dst = (unsigned)(uintptr_t)lds + g * GRPB + (w & 3) * 2048;
        const int tok = tid >> (ls & 31), r = tid & ((1 << (ls & 31)) - 1);
        const unsigned off = (ls >= 1 && tok < TP) ? (unsigned)(j * TP + tok) * (unsigned)H * 256u + (unsigned)(x + 8 * (r >> 1)) * 256u + (r & 1) * 128u : 0x7ffffff0u;
        asm volatile("s_mov_b32 m0, %0\n\ts_nop 3\n\tbuffer_load_dword %1, %2, 0 offen" PF_AUX " lds" :: "s"(dst), "v"(off), "s"(srdQ) : "memory");
    }
    LaneK lk;
    {
        const int ln = lane0, lr32 = ln & 31, lhi = ln >> 5, wq = w & 3;
        lk.dvoff = (unsigned)(8 * wq + ((ln >> 2) & 7)) * ((unsigned)H * 256u) +
                   (unsigned)(4 * (ln >> 5) + ((ln & 3) ^ (2 * (wq & 1) + ((ln >> 4) & 1)))) * 16u;
        const lds_cptr gb = (lds_cptr)lds + g * GRPB;
        lk.kb0 = gb + 2048 * (lr32 >> 3) + 64 * (lr32 & 7) + 16 * ((0 + lhi) ^ ((lr32 >> 2) & 3));
        lk.kb1 = gb + 2048 * (lr32 >> 3) + 64 * (lr32 & 7) + 16 * ((2 + lhi) ^ ((lr32 >> 2) & 3));
        const int vq = (ln & 15) >> 2, vp = ln & 3, vblk = (ln >> 4) & 1;
        lk.vb0 = gb + RINGB + 64 * (4 * lhi + vq) + 16 * ((2 * vblk + (vp >> 1)) ^ (0 + lhi)) + 8 * (vp & 1);
        lk.vb1 = gb + RINGB + 2048 + 64 * (4 * lhi + vq) + 16 * ((2 * vblk + (vp >> 1)) ^ (2 + lhi)) + 8 * (vp & 1);
        PIN(lk.dvoff); PIN(lk.kb0); PIN(lk.kb1); PIN(lk.vb0); PIN(lk.vb1);
    }
    SBAR();
    {
        const int tau = blockIdx.x;
        int bad = LPW > 64 || T < 16;
        const int lane = lane0;
        {
            const int sbase = (w * LPW + lane) * 8, sadj = max(min(sbase, T - 8), 0);
            asm volatile("s_waitcnt vmcnt(1)" : "+v"(sa), "+v"(sb), "+v"(sp) :: "memory");
            const int v[9] = {(int)sp, (int)sa[0], (int)sa[1], (int)sa[2], (int)sa[3], (int)sb[0], (int)sb[1], (int)sb[2], (int)sb[3]};
            unsigned tb[8]; bool dec = false;
#pragma unroll
            for (int j = 0; j < 8; ++j) {
                asm("v_xor_b32 %0, %1, %2\n\tv_min_u32 %0, 1, %0" : "=v"(tb[j]) : "v"(v[j + 1]), "v"(v[j]));
                dec |= v[j + 1] < v[j];
            }
            unsigned bits = ((tb[0] | (tb[1] << 1)) | ((tb[2] | (tb[3] << 1)) << 2)) | (((tb[4] | (tb[5] << 1)) | ((tb[6] | (tb[7] << 1)) << 2)) << 4);
            if (sadj == 0) bits |= 1u;
            const int shf = sbase - sadj;
            bits = (lane < LPW && shf < 8) ? (bits >> shf) << shf : 0u;
            const bool has = bits != 0;
            const unsigned long long hb = __ballot(has);
            const int rank = __builtin_amdgcn_mbcnt_hi((unsigned)(hb >> 32), __builtin_amdgcn_mbcnt_lo((unsigned)hb, 0));
            bad |= __any(dec || (bits & (bits - 1)) != 0) || __builtin_popcountll(hb) > 8;
            if (lane < 8) bl[63 - (8 * w + lane)] = T;
            if (has && rank < 8) bl[63 - (8 * w + rank)] = sadj + __builtin_ctz(bits);
            if (lane == 0) bl[64 + w] = bad;
        }
        asm volatile("s_waitcnt lgkmcnt(0)\n\ts_barrier" ::: "memory");
        const int start = bl[lane];
        const int fl = bl[64 + (lane & 7)];
        const int sh = __builtin_amdgcn_update_dpp(0x7fffffff, start, 0x138, 0xf, 0xf, false);
        const int end = min(scan_min(sh), T);
        const int len = max(end - start, 0), nq = (len + 127) >> 7, nk = (len + 31) >> 5;
        const bool heavy = nk > THETA;
        const int val = heavy ? nq : (nq << 16);
        const int incl = scan_add(val);
        const int tot = RDL(incl, 63);
        const int NH = tot & 0xffff, NL = tot >> 16;
        const int ntasks = (NH + ((NL + 1) >> 1)) * H;
        if (!__any(fl != 0) && ntasks <= G && hshift >= 0) {
            if (tau < ntasks) {
                const int exH = (incl - val) & 0xffff, exL = (incl - val) >> 16;
                const int head = tau & (H - 1), tix = tau >> hshift;
                Task t;
                t.head = head;
                if (tix < NH) {
                    const unsigned long long b = __ballot(heavy && exH <= tix && tix < exH + nq);
                    const int sidx = __builtin_ctzll(b);
                    const int st = RDL(start, sidx), en = RDL(end, sidx), nks = RDL(nk, sidx), ex = RDL(exH, sidx);
                    t.q0 = st + 128 * (tix - ex); t.qend = min(en, t.q0 + 128);
                    t.kfirst = st + 32 * g; t.kstride = 64; t.nunits = (nks - g + 1) >> 1; t.kend = en;
                    t.nsteps = (nks + 1) >> 1; t.merge = 1; t.nother = (nks - (g ^ 1) + 1) >> 1;
                } else {
                    const int pi = tix - NH;
                    const int it0 = pi, it1 = NL - 1 - pi;
                    const unsigned long long b0 = __ballot(!heavy && exL <= it0 && it0 < exL + nq);
                    const unsigned long long b1 = __ballot(!heavy && exL <= it1 && it1 < exL + nq);
                    const int s0 = __builtin_ctzll(b0), s1 = __builtin_ctzll(b1);
                    const int n0 = RDL(nk, s0), n1 = it1 != it0 ? RDL(nk, s1) : 0;
                    const int sidx = g ? s1 : s0, it = g ? it1 : it0;
                    const int st = RDL(start, sidx), en = RDL(end, sidx), ex = RDL(exL, sidx);
                    t.q0 = st + 128 * (it - ex); t.qend = min(en, t.q0 + 128);
                    t.kfirst = st; t.kstride = 32; t.nunits = g ? n1 : n0; t.kend = en;
                    t.nsteps = max(n0, n1); t.merge = 0; t.nother = 0;
                }
                run_task<false>(t, Q, K, V, seg, O, T, H, lds, w, lk);
            }
            goto finish;
        }
        asm volatile("s_waitcnt vmcnt(0) lgkmcnt(0)" ::: "memory");
        __syncthreads();
    }
    {
        int* sc = (int*)lds;
        if (tid == 0) sc[0] = 0;
        __syncthreads();
        for (int p = tid + 1; p < T; p += 512) if (seg[p] < seg[p - 1]) sc[0] = 1;
        __syncthreads();
        const bool unsorted = RFL(sc[0]) != 0;
        __syncthreads();
        const int ntasks = ((T + 127) >> 7) * H;
        for (int tau = blockIdx.x; tau < ntasks; tau += G) {
            const int head = tau % H, tix = tau / H;
            const int q0 = tix * 128, qend = min(T, q0 + 128);
            int klo = 0, khi = T;
            if (!unsorted) {
                if (tid == 0) { sc[2] = T; sc[3] = 0; }
                __syncthreads();
                const int sa2 = seg[q0], sb2 = seg[qend - 1];
                int lo = T, hi2 = 0;
                for (int p = tid; p < T; p += 512) { const int a = seg[p]; if (a == sa2) lo = min(lo, p); if (a == sb2) hi2 = max(hi2, p + 1); }
                atomicMin(&sc[2], lo); atomicMax(&sc[3], hi2);
                __syncthreads();
                klo = RFL(sc[2]); khi = RFL(sc[3]);
                __syncthreads();
            }
            const int nks = (khi - klo + 31) >> 5;
            Task t;
            t.head = head; t.q0 = q0; t.qend = qend; t.kfirst = klo + 32 * g; t.kstride = 64; t.nunits = (nks - g + 1) >> 1;
            t.kend = khi; t.nsteps = (nks + 1) >> 1; t.merge = 1; t.nother = (nks - (g ^ 1) + 1) >> 1;
            run_task<true>(t, Q, K, V, seg, O, T, H, lds, w, lk);
            __syncthreads();
        }
    }
finish:;
}

extern "C" void kernel_launch(void* const* d_in, const int* in_sizes, int n_in, void* d_out, int out_size, void* d_ws, size_t ws_size,
                              hipStream_t stream) {
    const int T = in_sizes[3];
    const int H = in_sizes[0] / (T * 128);
    int hshift = -1;
    for (int s = 0; s < 16; ++s) if ((1 << s) == H) hshift = s;
    (void)hipFuncSetAttribute((const void*)ragged_attn_kernel, hipFuncAttributeMaxDynamicSharedMemorySize, LDS_BYTES);
    hipLaunchKernelGGL(ragged_attn_kernel, dim3(256), dim3(512), LDS_BYTES, stream,
                       (const char*)d_in[0], (const char*)d_in[1], (const char*)d_in[2], (const int*)d_in[3], (char*)d_out, T, H, hshift
                       );
}
```
